```python
import torch

def _softmax(x, axis=-1):
    return torch.softmax(x, dim=axis)

def _gelu(x):
    return torch.nn.functional.gelu(x, approximate='tanh')

def _silu(x):
    return torch.nn.functional.silu(x)

def _sigmoid(x):
    return torch.sigmoid(x)

def _layernorm(x, axis=-1, eps=1e-05):
    m = x.mean(dim=axis, keepdim=True)
    v = x.var(dim=axis, keepdim=True, unbiased=False)
    return (x - m) / torch.sqrt(v + eps)

def _scatter_add(tgt, idx, src, axis=0):
    return tgt.clone().index_add_(axis, idx, src)

def _segment_sum(src, seg, n):
    out = torch.zeros((n,) + src.shape[1:], dtype=src.dtype)
    return out.index_add_(0, seg, src)

def _pad(x, pw):
    flat = tuple((v for pair in reversed(pw) for v in pair))
    return torch.nn.functional.pad(x, flat)
kernel_name = 'adaLN_cond__gqa_block_cached__geglu_ffn__decode'

def setup_inputs():

    _w = lambda a: (lambda t: t.to(torch.bfloat16) if t.is_floating_point() else t)(torch.as_tensor(a))

    def _A_setup():
        B, Sq, D = (8, 1, 576)
        Hq, Hkv, Sk, Dh = (32, 16, 8192, 128)
        assert Hq % Hkv == 0, (Hq, Hkv)
        return {'x': _w((lambda *s: torch.randn(*s, dtype=torch.bfloat16) * 0.1)(B, Sq, D)), 'k_cache': _w((lambda *s: torch.randn(*s, dtype=torch.bfloat16) * 0.8)(B, Hkv, Sk, Dh)), 'v_cache': _w((lambda *s: torch.randn(*s, dtype=torch.bfloat16) * 0.8)(B, Hkv, Sk, Dh)), 'wq': _w((lambda *s: torch.randn(*s, dtype=torch.bfloat16) * 0.8)(D, Hq * Dh) * D ** (-0.5)), 'wo': _w((lambda *s: torch.randn(*s, dtype=torch.bfloat16) * 0.8)(Hq * Dh, D) * (Hq * Dh) ** (-0.5))}
    _A_in = _A_setup()

    def _B_setup():
        B, S, D, F = (8, 1, 576, 4608)
        return {'x': _w((lambda *s: torch.randn(*s, dtype=torch.bfloat16) * 0.1)(B, S, D)), 'w_gate': _w((lambda *s: torch.randn(*s, dtype=torch.bfloat16) * 0.1)(D, F)), 'w_up': _w((lambda *s: torch.randn(*s, dtype=torch.bfloat16) * 0.1)(D, F)), 'w_down': _w((lambda *s: torch.randn(*s, dtype=torch.bfloat16) * 0.1)(F, D))}
    _B_in = _B_setup()
    return {'A_x': _A_in['x'], 'A_k_cache': _A_in['k_cache'], 'A_v_cache': _A_in['v_cache'], 'A_wq': _A_in['wq'], 'A_wo': _A_in['wo'], 'B_x': _B_in['x'], 'B_w_gate': _B_in['w_gate'], 'B_w_up': _B_in['w_up'], 'B_w_down': _B_in['w_down'], 'c': _w((lambda *s: torch.randn(*s, dtype=torch.bfloat16) * 0.4)(8, 576) * 8.0), 'w_mod': _w((lambda *s: torch.randn(*s, dtype=torch.bfloat16) * 0.4)(576, 6 * 576) * 576 ** (-0.5) * 8.0)}

def reference(A_x, A_k_cache, A_v_cache, A_wq, A_wo, B_x, B_w_gate, B_w_up, B_w_down, c, w_mod):

    def _A_ref(x, k_cache, v_cache, wq, wo):
        B, Sq, D = x.shape
        Hq, Dh = (32, 128)
        Hkv = k_cache.shape[1]
        rep = Hq // Hkv
        q = (lambda x, *d: x.permute(*d))((x @ wq).reshape(B, Sq, Hq, Dh), 0, 2, 1, 3)
        k = (lambda x, n, axis: torch.repeat_interleave(x, n, dim=axis))(k_cache, rep, axis=1)
        v = (lambda x, n, axis: torch.repeat_interleave(x, n, dim=axis))(v_cache, rep, axis=1)
        scores = torch.einsum('bhqd,bhkd->bhqk', q, k) * Dh ** (-0.5)
        p = _softmax(scores, axis=-1)
        o = torch.einsum('bhqk,bhkd->bhqd', p, v)
        return (lambda x, *d: x.permute(*d))(o, 0, 2, 1, 3).reshape(B, Sq, Hq * Dh) @ wo

    def _B_ref(x, w_gate, w_up, w_down):
        return _gelu(x @ w_gate) * (x @ w_up) @ w_down
    x = A_x
    mod = (_silu(c) @ w_mod).reshape(c.shape[0], 6, 1, -1)
    sa, ca, ga, sb, cb, gb = (mod[:, 0].clone(), mod[:, 1].clone(), mod[:, 2].clone(), mod[:, 3].clone(), mod[:, 4].clone(), mod[:, 5].clone())
    h = _layernorm(x, axis=-1) * (1 + ca) + sa
    x = x + ga * _A_ref(h, A_k_cache, A_v_cache, A_wq, A_wo)
    h = _layernorm(x, axis=-1) * (1 + cb) + sb
    x = x + gb * _B_ref(h, B_w_gate, B_w_up, B_w_down)
    return x
```

```cpp
#include <hip/hip_runtime.h>
#include <hip/hip_bf16.h>
#include <cstdint>

using bf16 = __hip_bfloat16;
using bf16x2 = __attribute__((ext_vector_type(2))) __bf16;
using bf16x8 = __attribute__((ext_vector_type(8))) __bf16;
using s16x4 = __attribute__((ext_vector_type(4))) short;
using s16x8 = __attribute__((ext_vector_type(8))) short;
using u32x4 = __attribute__((ext_vector_type(4))) unsigned;
using f32x4 = __attribute__((ext_vector_type(4))) float;
typedef unsigned long long ull;
#define GAS __attribute__((address_space(1)))
#define LAS __attribute__((address_space(3)))

constexpr int NB = 8, DM = 576, HQ = 32, HKV = 16, SK = 8192, DH = 128, FF = 4608, NMOD = 6 * DM;
constexpr float SCALE = 0.08838834764831845f;
constexpr float LOG2E = 1.4426950408889634f;
constexpr unsigned TAG = 0x5EEDC0DEu;

constexpr int PREC = 132;
constexpr int GQ = 0;
constexpr int GP = GQ + NB * 2048;
constexpr int GW = GP + NB * HQ * 2 * PREC;
constexpr int GX = GW + 32 * NB * DM;
constexpr int GA = GX + NB * 288;
constexpr int GD = GA + NB * 2304;
constexpr int GT = GD + 24 * NB * DM;
constexpr int GEND = GT + 16;
static_assert(GEND % 2 == 0, "16-byte clear");
constexpr size_t WS_MOD = (size_t)GEND * 8;

__device__ __forceinline__ float bfr(float x) { return (float)(__bf16)x; }
__device__ __forceinline__ float bflo(unsigned u) { return __builtin_bit_cast(float, u << 16); }
__device__ __forceinline__ float bfhi(unsigned u) { return __builtin_bit_cast(float, u & 0xffff0000u); }
__device__ __forceinline__ unsigned pk2(float lo, float hi) { return (__float_as_uint(lo) >> 16) | (__float_as_uint(hi) & 0xffff0000u); }
__device__ __forceinline__ float dot2(unsigned k, unsigned q, float acc) {
  return __builtin_amdgcn_fdot2_f32_bf16(__builtin_bit_cast(bf16x2, k), __builtin_bit_cast(bf16x2, q), acc, false);
}
template <int CTRL> __device__ __forceinline__ float dpp(float x) {
  return __builtin_bit_cast(float, __builtin_amdgcn_mov_dpp(__builtin_bit_cast(int, x), CTRL, 0xf, 0xf, true));
}
constexpr int XOR1 = 0xB1, XOR2 = 0x4E, XOR7 = 0x141, XOR8 = 0x128, ROR4 = 0x124;
__device__ __forceinline__ float tr16(float lo, float hi) {
  auto s = __builtin_amdgcn_permlane16_swap(__float_as_uint(lo), __float_as_uint(hi), false, false);
  return __uint_as_float(s[0]) + __uint_as_float(s[1]);
}
__device__ __forceinline__ float tr32(float lo, float hi) {
  auto s = __builtin_amdgcn_permlane32_swap(__float_as_uint(lo), __float_as_uint(hi), false, false);
  return __uint_as_float(s[0]) + __uint_as_float(s[1]);
}
__device__ __forceinline__ float xrow16_max(float x) {
  auto s = __builtin_amdgcn_permlane16_swap(__float_as_uint(x), __float_as_uint(x), false, false);
  x = fmaxf(__uint_as_float(s[0]), __uint_as_float(s[1]));
  auto t = __builtin_amdgcn_permlane32_swap(__float_as_uint(x), __float_as_uint(x), false, false);
  return fmaxf(__uint_as_float(t[0]), __uint_as_float(t[1]));
}
__device__ __forceinline__ float xrow16_sum(float x) { x = tr16(x, x); return tr32(x, x); }
__device__ __forceinline__ float wave_sum(float s) {
  s += dpp<XOR1>(s); s += dpp<XOR2>(s); s += dpp<XOR7>(s); s += dpp<XOR8>(s); return xrow16_sum(s);
}
__device__ __forceinline__ void st_gr(ull* g, unsigned v) {
  __hip_atomic_store((GAS ull*)g, ((ull)TAG << 32) | (ull)v, __ATOMIC_RELAXED, __HIP_MEMORY_SCOPE_AGENT);
}
__device__ __forceinline__ ull ld_gr(const ull* g) { return __hip_atomic_load((GAS ull*)g, __ATOMIC_RELAXED, __HIP_MEMORY_SCOPE_AGENT); }
__device__ __forceinline__ bool spin_fail(unsigned& spins, ull* gr) {
  __builtin_amdgcn_s_sleep(2);
  ++spins;
  if (spins > (1u << 18)) { __hip_atomic_store((GAS ull*)(gr + GT), 1ull, __ATOMIC_RELAXED, __HIP_MEMORY_SCOPE_AGENT); return true; }
  if ((spins & 1023u) == 0u) return __builtin_amdgcn_readfirstlane((unsigned)ld_gr(gr + GT)) != 0u;
  return false;
}
template <int N, typename IDX>
__device__ __forceinline__ void gather(ull* gr, IDX idx, unsigned (&v)[N]) {
  unsigned spins = 0;
  for (;;) {
    bool ok = true;
#pragma unroll
    for (int i = 0; i < N; ++i) { const int id = idx(i); if (id >= 0) { const ull g = ld_gr(gr + id); v[i] = (unsigned)g; ok &= (unsigned)(g >> 32) == TAG; } }
    if (__all(ok)) break;
    if (spin_fail(spins, gr)) break;
  }
}
__device__ __forceinline__ void glds16(const void* gsrc, unsigned lds_dst) {
  unsigned keep;
  asm volatile("s_mov_b32 %0, m0\n\ts_mov_b32 m0, %2\n\ts_nop 0\n\tglobal_load_lds_dwordx4 %1, off\n\ts_mov_b32 m0, %0" : "=&s"(keep) : "v"(gsrc), "s"(lds_dst) : "memory");
}
__device__ __forceinline__ s16x4 trd(const char* p) {
  return __builtin_bit_cast(s16x4, __builtin_amdgcn_ds_read_tr16_b64_v4i16((LAS s16x4*)p));
}
template <int NK>
__device__ __forceinline__ f32x4 mm_tile(const char* img, int RS, const char* act, int AS, int lane) {
  const int g = lane >> 4, q = (lane & 15) >> 2, p = lane & 3;
  const char* pb = img + (8 * g + q) * RS + 8 * p;
  const char* pa = act + (lane & 7) * AS + g * 16;
  f32x4 acc = {0.f, 0.f, 0.f, 0.f};
#pragma unroll
  for (int ks = 0; ks < NK; ++ks) {
    const bf16x8 a = *reinterpret_cast<const bf16x8*>(pa + ks * 64);
    const s16x4 lo = trd(pb + (ks * 32) * RS), hi = trd(pb + (ks * 32 + 4) * RS);
    const s16x8 bb = {lo[0], lo[1], lo[2], lo[3], hi[0], hi[1], hi[2], hi[3]};
    acc = __builtin_amdgcn_mfma_f32_16x16x32_bf16(a, __builtin_bit_cast(bf16x8, bb), acc, 0, 0, 0);
  }
  return acc;
}
__device__ __forceinline__ void ln_row(const unsigned (&xp)[5], const unsigned* ms  , unsigned* dst, int lane) {
  float s = 0.f;
#pragma unroll
  for (int i = 0; i < 5; ++i) if (lane + 64 * i < 288) s += bflo(xp[i]) + bfhi(xp[i]);
  const float mean = wave_sum(s) * (1.0f / DM);
  float vs = 0.f;
#pragma unroll
  for (int i = 0; i < 5; ++i) if (lane + 64 * i < 288) { const float d0 = bflo(xp[i]) - mean, d1 = bfhi(xp[i]) - mean; vs += d0 * d0 + d1 * d1; }
  const float var = bfr(wave_sum(vs) * (1.0f / DM));
  const float mb = bfr(mean);
  const float den = bfr(sqrtf(bfr(var + 1e-5f)));
#pragma unroll
  for (int i = 0; i < 5; ++i) if (lane + 64 * i < 288) {
    const unsigned sh = ms[lane + 64 * i], sc = ms[288 + lane + 64 * i];
    const float h0 = bfr(bfr(bfr(bfr(bflo(xp[i]) - mb) / den) * bfr(1.0f + bflo(sc))) + bflo(sh));
    const float h1 = bfr(bfr(bfr(bfr(bfhi(xp[i]) - mb) / den) * bfr(1.0f + bfhi(sc))) + bfhi(sh));
    dst[lane + 64 * i] = pk2(h0, h1);
  }
}

constexpr int AS = 1168;
__global__ void __launch_bounds__(512) k_pre(const bf16* cnd, const bf16* wmod, unsigned* modp, u32x4* gr16) {
  __shared__ __attribute__((aligned(16))) char lds[72 * 1024 + 8 * AS];
  const int tid = threadIdx.x, lane = tid & 63, wave = __builtin_amdgcn_readfirstlane(tid >> 6), mc = blockIdx.x;
  const unsigned lbase = (unsigned)(uintptr_t)lds;
  unsigned xp[5];
  if (mc < 54) {
#pragma unroll
    for (int i = 0; i < 9; ++i) { const int u = wave + 8 * i; glds16(wmod + (size_t)(u * 8 + (lane >> 3)) * NMOD + mc * 64 + (lane & 7) * 8, lbase + u * 1024); }
    const unsigned* s32 = reinterpret_cast<const unsigned*>(cnd) + wave * 288;
#pragma unroll
    for (int i = 0; i < 5; ++i) xp[i] = (lane + 64 * i < 288) ? s32[lane + 64 * i] : 0u;
  }
  for (int i = blockIdx.x * 512 + tid; i < GEND / 2; i += 256 * 512) gr16[i] = u32x4{0u, 0u, 0u, 0u};
  if (mc >= 54) return;
  unsigned* actp = reinterpret_cast<unsigned*>(lds + 72 * 1024);
#pragma unroll
  for (int i = 0; i < 5; ++i) if (lane + 64 * i < 288) { const float v0 = bflo(xp[i]), v1 = bfhi(xp[i]);
    actp[wave * (AS / 4) + lane + 64 * i] = pk2(bfr(v0 / (1.0f + __expf(-v0))), bfr(v1 / (1.0f + __expf(-v1)))); }
  asm volatile("s_waitcnt vmcnt(0)" ::: "memory");
  __syncthreads();
  if (wave < 4) {
    const f32x4 y = mm_tile<18>(lds + wave * 32, 128, lds + 72 * 1024, AS, lane);
#pragma unroll
    for (int e = 0; e < 4; ++e) { const float v = bfr(y[e]), n = dpp<XOR1>(v);
      if (lane < 32 && (lane & 1) == 0) modp[((lane >> 4) * 4 + e) * 1728 + mc * 32 + wave * 8 + ((lane & 15) >> 1)] = pk2(v, n); }
  }
}

constexpr int LW = 0, LG = LW + 18 * 1024, LD = LG + 54 * 1024, LA = LD + 27 * 1024, LB = LA + 8 * AS, LS = LB + 8 * AS, LC = LS + NB * 576 * 4, LEND = LC + 2048;

__global__ void __launch_bounds__(512, 2) k_main(const bf16* x, const bf16* K, const bf16* V, const bf16* wq, const bf16* wo, const bf16* wg, const bf16* wu,
                                                 const bf16* wd, const unsigned* mod, ull* gr, bf16* out) {
  __shared__ __attribute__((aligned(16))) char lds[LEND];
  const int j = blockIdx.x, xcd = j & 7, slot = j >> 3;
  const int tid = threadIdx.x, lane = tid & 63, wave = __builtin_amdgcn_readfirstlane(tid >> 6), r = lane >> 4, c = lane & 15;
  const unsigned lbase = (unsigned)(uintptr_t)lds;
  const int b = j >> 5, kvh = (j >> 1) & 15, sp = j & 1;
  const int wh = xcd * 4 + (slot >> 3), wcc = slot & 7;
  const bool ffn = slot < 24;
  const int gi = xcd * 24 + slot;
  const int kc = xcd * 3 + (slot >> 3), dcc = slot & 7;
  const int qc = xcd * 8 + (slot - 24);
  const int rb = j >> 5, rn0 = (j & 31) * 18;
  unsigned* actp = reinterpret_cast<unsigned*>(lds + LB);
  unsigned* lsp = reinterpret_cast<unsigned*>(lds + LS);
  if (!ffn) {
    const bf16* src = wq + qc * 64; const int ld = HQ * DH;
#pragma unroll
    for (int i = 0; i < 9; ++i) { const int u = wave + 8 * i; glds16(src + (size_t)(u * 8 + (lane >> 3)) * ld + (lane & 7) * 8, lbase + LG + u * 1024); }
  }
  unsigned xp[5], mv[9];
  if (!ffn) {
    const unsigned* s32 = reinterpret_cast<const unsigned*>(x) + wave * 288;
#pragma unroll
    for (int i = 0; i < 5; ++i) xp[i] = (lane + 64 * i < 288) ? s32[lane + 64 * i] : 0u;
#pragma unroll
    for (int i = 0; i < 9; ++i) mv[i] = mod[wave * 1728 + lane + 64 * i];
  }
  __builtin_amdgcn_sched_barrier(0);
  const int ntile = sp ? 15 : 17;
  const size_t base = ((size_t)(b * HKV + kvh) * SK + (size_t)sp * (17 * 256) + wave * 32 + r) * DH + 8 * c;
  const bf16* kp = K + base;
  const bf16* vp = V + base;
  u32x4 KA[8], VA[8], KB[8], VB[8];
#define ISSUE(KX, VX, it_) do { _Pragma("unroll") for (int jj = 0; jj < 8; ++jj) { \
    KX[jj] = __builtin_nontemporal_load(reinterpret_cast<const u32x4*>(kp + (size_t)(it_) * (256 * DH) + jj * 4 * DH)); \
    VX[jj] = __builtin_nontemporal_load(reinterpret_cast<const u32x4*>(vp + (size_t)(it_) * (256 * DH) + jj * 4 * DH)); } } while (0)
  if (!ffn) {
#pragma unroll
    for (int i = 0; i < 9; ++i) lsp[wave * 576 + lane + 64 * i] = mv[i];
    ln_row(xp, lsp + wave * 576, actp + wave * (AS / 4), lane);
    asm volatile("s_waitcnt vmcnt(0)" ::: "memory");
    __syncthreads();
    if (wave < 4) {
      const f32x4 y = mm_tile<18>(lds + LG + wave * 32, 128, lds + LB, AS, lane);
#pragma unroll
      for (int e = 0; e < 4; ++e) { const float v2 = bfr(y[e]), n = dpp<XOR1>(v2);
        if (lane < 32 && (lane & 1) == 0) st_gr(gr + GQ + ((lane >> 4) * 4 + e) * 2048 + qc * 32 + wave * 8 + ((lane & 15) >> 1), pk2(v2, n)); }
    }
    __syncthreads();
  }
  __builtin_amdgcn_sched_barrier(0);
  ISSUE(KA, VA, 0);
  ISSUE(KB, VB, 1);
  __builtin_amdgcn_sched_barrier(0);
  for (int u = wave; u < 18; u += 8) { const int p = u * 64 + lane;
    glds16(wo + (size_t)(wh * 128 + p / 9) * DM + wcc * 72 + (p % 9) * 8, lbase + LW + u * 1024); }
  if (ffn) {
    for (int u = wave; u < 54; u += 8) { const int p = u * 64 + lane, sub = p % 6; const bf16* src = sub < 3 ? wg : wu;
      glds16(src + (size_t)(p / 6) * FF + gi * 24 + (sub % 3) * 8, lbase + LG + u * 1024); }
    for (int u = wave; u < 27; u += 8) { const int p = u * 64 + lane;
      glds16(wd + (size_t)(kc * 192 + p / 9) * DM + dcc * 72 + (p % 9) * 8, lbase + LD + u * 1024); }
  }
  if (ffn) {
#pragma unroll
    for (int i = 0; i < 9; ++i) lsp[wave * 576 + lane + 64 * i] = mod[wave * 1728 + 864 + lane + 64 * i];
  }
  unsigned gab[1] = {0u}, gbb[1] = {0u}, xres = 0u;
  if (tid < 9) { xres = reinterpret_cast<const unsigned*>(x)[rb * 288 + rn0 / 2 + tid]; gab[0] = mod[rb * 1728 + 576 + rn0 / 2 + tid]; gbb[0] = mod[rb * 1728 + 1440 + rn0 / 2 + tid]; }
  unsigned qpk[2][4];
  {
    const ull* qg = gr + GQ + b * 2048 + kvh * 128 + 4 * c;
    unsigned spins = 0;
    for (;;) {
      bool ok = true;
#pragma unroll
      for (int g = 0; g < 2; ++g)
#pragma unroll
        for (int k = 0; k < 4; ++k) { const ull v = ld_gr(qg + g * 64 + k); qpk[g][k] = (unsigned)v; ok &= (unsigned)(v >> 32) == TAG; }
      if (__all(ok)) break;
      if (spin_fail(spins, gr)) break;
    }
  }
  float m = -INFINITY, l = 0.f, acc[2][8];
#pragma unroll
  for (int g = 0; g < 2; ++g)
#pragma unroll
    for (int k = 0; k < 8; ++k) acc[g][k] = 0.f;
#define PV(VX, jj, g) do { const float pb = __builtin_bit_cast(float, __builtin_amdgcn_update_dpp(0, __builtin_bit_cast(int, p), 0x150 + 2 * (jj) + (g), 0xf, 0xf, false)); \
      _Pragma("unroll") for (int k = 0; k < 8; ++k) acc[g][k] = fmaf(pb, vf[k], acc[g][k]); } while (0)
#define PVJ(VX, jj) do { float vf[8]; _Pragma("unroll") for (int k = 0; k < 4; ++k) { vf[2 * k] = bflo(VX[jj][k]); vf[2 * k + 1] = bfhi(VX[jj][k]); } PV(VX, jj, 0); PV(VX, jj, 1); } while (0)
#define TR_STEP(HALF, CTRL, BIT) _Pragma("unroll") for (int h = 0; h < (HALF); ++h) { \
      const float lo_ = xx[h], hi_ = xx[h + (HALF)]; const float t1_ = lo_ + dpp<CTRL>(lo_), t2_ = hi_ + dpp<CTRL>(hi_); xx[h] = (c & (BIT)) ? t2_ : t1_; }
#define STEP(KX, VX) do { \
    float xx[16]; \
    _Pragma("unroll") for (int jj = 0; jj < 8; ++jj) _Pragma("unroll") for (int g = 0; g < 2; ++g) { float d = 0.f; \
        _Pragma("unroll") for (int k = 0; k < 4; ++k) d = dot2(KX[jj][k], qpk[g][k], d); xx[jj * 2 + g] = d; } \
    TR_STEP(8, XOR8, 8) TR_STEP(4, XOR7, 4) TR_STEP(2, XOR2, 2) TR_STEP(1, XOR1, 1) \
    const float sc = bfr(bfr(xx[0]) * SCALE) * LOG2E; \
    float pm = sc; \
    pm = fmaxf(pm, dpp<XOR2>(pm)); pm = fmaxf(pm, dpp<ROR4>(pm)); pm = fmaxf(pm, dpp<XOR8>(pm)); pm = xrow16_max(pm); \
    const float mn = fmaxf(m, pm); \
    const float alpha = __builtin_amdgcn_exp2f(m - mn), p = __builtin_amdgcn_exp2f(sc - mn); \
    l = fmaf(l, alpha, p); m = mn; \
    const float aother = dpp<XOR1>(alpha); \
    const float a0 = (c & 1) ? aother : alpha, a1 = (c & 1) ? alpha : aother; \
    _Pragma("unroll") for (int k = 0; k < 8; ++k) { acc[0][k] *= a0; acc[1][k] *= a1; } \
    PVJ(VX, 0); PVJ(VX, 1); PVJ(VX, 2); PVJ(VX, 3); PVJ(VX, 4); PVJ(VX, 5); PVJ(VX, 6); PVJ(VX, 7); } while (0)
  for (int it = 0; it + 3 < ntile; it += 2) {
    STEP(KA, VA);
    __builtin_amdgcn_sched_barrier(0);
    ISSUE(KA, VA, it + 2);
    __builtin_amdgcn_sched_barrier(0);
    STEP(KB, VB);
    __builtin_amdgcn_sched_barrier(0);
    ISSUE(KB, VB, it + 3);
    __builtin_amdgcn_sched_barrier(0);
  }
  STEP(KA, VA);
  __builtin_amdgcn_sched_barrier(0);
  ISSUE(KA, VA, ntile - 1);
  __builtin_amdgcn_sched_barrier(0);
  STEP(KB, VB);
  __builtin_amdgcn_sched_barrier(0);
  STEP(KA, VA);
#pragma unroll
  for (int g = 0; g < 2; ++g)
#pragma unroll
    for (int k = 0; k < 8; ++k) acc[g][k] = xrow16_sum(acc[g][k]);
  { float t = l; t += dpp<XOR2>(t); t += dpp<ROR4>(t); t += dpp<XOR8>(t); l = xrow16_sum(t); }
  float* mrg = reinterpret_cast<float*>(lds + LA);
  if (r == 0) {
#pragma unroll
    for (int g = 0; g < 2; ++g) {
      float* rec = mrg + (wave * 2 + g) * PREC;
      *reinterpret_cast<f32x4*>(rec + 4 + 8 * c) = f32x4{acc[g][0], acc[g][1], acc[g][2], acc[g][3]};
      *reinterpret_cast<f32x4*>(rec + 8 + 8 * c) = f32x4{acc[g][4], acc[g][5], acc[g][6], acc[g][7]};
      if (c == g) { rec[0] = m; rec[1] = l; }
    }
  }
  asm volatile("s_waitcnt vmcnt(0)" ::: "memory");
  __syncthreads();
  if (tid < 256) {
    const int g = tid >> 7, d = tid & 127;
    float M = -INFINITY;
#pragma unroll
    for (int w = 0; w < 8; ++w) M = fmaxf(M, mrg[(w * 2 + g) * PREC]);
    float L = 0.f, o = 0.f;
#pragma unroll
    for (int w = 0; w < 8; ++w) { const float* rec = mrg + (w * 2 + g) * PREC; const float wt = __builtin_amdgcn_exp2f(rec[0] - M); L = fmaf(wt, rec[1], L); o = fmaf(wt, rec[4 + d], o); }
    ull* dst = gr + GP + ((b * HQ + kvh * 2 + g) * 2 + sp) * PREC;
    st_gr(dst + d, __float_as_uint(o));
    if (d < 2) st_gr(dst + 128 + d, __float_as_uint(d == 0 ? M : L));
  }
  float* raw = reinterpret_cast<float*>(lds + LA);
  {
    unsigned v[5];
    gather<5>(gr, [&](int i) { const int idx = tid + 512 * i, bb = idx / 264, e = idx % 264; return (idx < 2112 && (e % 132) < 130) ? GP + (bb * HQ + wh) * 2 * PREC + e : -1; }, v);
    __syncthreads();
#pragma unroll
    for (int i = 0; i < 5; ++i) { const int idx = tid + 512 * i; if (idx < 2112 && ((idx % 264) % 132) < 130) raw[idx] = __uint_as_float(v[i]); }
  }
  __syncthreads();
  {
    const int k2 = lane, bb = wave;
    const float* p0 = raw + bb * 264; const float* p1 = p0 + 132;
    const float M = fmaxf(p0[128], p1[128]);
    const float w0 = __builtin_amdgcn_exp2f(p0[128] - M), w1 = __builtin_amdgcn_exp2f(p1[128] - M);
    const float L = w0 * p0[129] + w1 * p1[129];
    actp[bb * (AS / 4) + k2] = pk2(bfr((w0 * p0[2 * k2] + w1 * p1[2 * k2]) / L), bfr((w0 * p0[2 * k2 + 1] + w1 * p1[2 * k2 + 1]) / L));
  }
  __syncthreads();
  if (wave < 5) {
    const f32x4 y = mm_tile<4>(lds + LW + wave * 32, 144, lds + LB, AS, lane);
    const int col = wave * 16 + (lane & 15);
    if (lane < 32 && col < 72) {
#pragma unroll
      for (int e = 0; e < 4; ++e) st_gr(gr + GW + (wh * NB + (lane >> 4) * 4 + e) * DM + wcc * 72 + col, __float_as_uint(y[e]));
    }
  }
  unsigned x1p = 0u;
  {
    unsigned v[2];
    gather<2>(gr, [&](int i) { const int idx = tid + 512 * i; return idx < 576 ? GW + ((idx / 18) * NB + rb) * DM + rn0 + idx % 18 : -1; }, v);
    __syncthreads();
    raw[tid] = __uint_as_float(v[0]);
    if (tid < 64) raw[512 + tid] = __uint_as_float(v[1]);
    __syncthreads();
    if (tid < 9) {
      float s0 = 0.f, s1 = 0.f;
#pragma unroll
      for (int h2 = 0; h2 < 32; ++h2) { s0 += raw[h2 * 18 + 2 * tid]; s1 += raw[h2 * 18 + 2 * tid + 1]; }
      x1p = pk2(bfr(bflo(xres) + bfr(bflo(gab[0]) * bfr(s0))), bfr(bfhi(xres) + bfr(bfhi(gab[0]) * bfr(s1))));
      st_gr(gr + GX + rb * 288 + rn0 / 2 + tid, x1p);
    }
  }
  if (ffn) {
    {
      unsigned v[5];
      gather<5>(gr, [&](int i) { return lane + 64 * i < 288 ? GX + wave * 288 + lane + 64 * i : -1; }, v);
      ln_row(v, lsp + wave * 576, actp + wave * (AS / 4), lane);
    }
    __syncthreads();
    float* gu = reinterpret_cast<float*>(lds + LC);
    if (wave < 3) {
      const f32x4 y = mm_tile<18>(lds + LG + wave * 32, 96, lds + LB, AS, lane);
      if (lane < 32) {
#pragma unroll
        for (int e = 0; e < 4; ++e) gu[((lane >> 4) * 4 + e) * 48 + wave * 16 + (lane & 15)] = y[e];
      }
    }
    __syncthreads();
    if (tid < 96) {
      const int bb = tid / 12, cp = tid % 12;
      float a[2];
#pragma unroll
      for (int e = 0; e < 2; ++e) { const int col = 2 * cp + e;
        const float g = bfr(gu[bb * 48 + col]), u = bfr(gu[bb * 48 + 24 + col]);
        const float t = tanhf(0.7978845608028654f * (g + 0.044715f * g * g * g));
        a[e] = bfr(bfr(0.5f * g * (1.0f + t)) * u); }
      st_gr(gr + GA + bb * 2304 + gi * 12 + cp, pk2(a[0], a[1]));
    }
    {
      unsigned v[2];
      gather<2>(gr, [&](int i) { return (i == 0 || lane < 32) ? GA + wave * 2304 + kc * 96 + lane + 64 * i : -1; }, v);
      actp[wave * (AS / 4) + lane] = v[0];
      if (lane < 32) actp[wave * (AS / 4) + 64 + lane] = v[1];
    }
    __syncthreads();
    if (wave < 5) {
      const f32x4 y = mm_tile<6>(lds + LD + wave * 32, 144, lds + LB, AS, lane);
      const int col = wave * 16 + (lane & 15);
      if (lane < 32 && col < 72) {
#pragma unroll
        for (int e = 0; e < 4; ++e) st_gr(gr + GD + (kc * NB + (lane >> 4) * 4 + e) * DM + dcc * 72 + col, __float_as_uint(y[e]));
      }
    }
  }
  {
    unsigned v[1];
    gather<1>(gr, [&](int) { return tid < 432 ? GD + ((tid / 18) * NB + rb) * DM + rn0 + tid % 18 : -1; }, v);
    __syncthreads();
    raw[tid] = __uint_as_float(v[0]);
    __syncthreads();
    if (tid < 9) {
      float s0 = 0.f, s1 = 0.f;
#pragma unroll
      for (int k2 = 0; k2 < 24; ++k2) { s0 += raw[k2 * 18 + 2 * tid]; s1 += raw[k2 * 18 + 2 * tid + 1]; }
      reinterpret_cast<unsigned*>(out)[rb * 288 + rn0 / 2 + tid] = pk2(bfr(bflo(x1p) + bfr(bflo(gbb[0]) * bfr(s0))), bfr(bfhi(x1p) + bfr(bfhi(gbb[0]) * bfr(s1))));
    }
  }
}

extern "C" void kernel_launch(void* const* d_in, const int* in_sizes, int n_in, void* d_out, int out_size, void* d_ws, size_t ws_size, hipStream_t stream) {
  const bf16* x = (const bf16*)d_in[0]; const bf16* K = (const bf16*)d_in[1]; const bf16* V = (const bf16*)d_in[2];
  const bf16* wq = (const bf16*)d_in[3]; const bf16* wo = (const bf16*)d_in[4];
  const bf16* wg = (const bf16*)d_in[6]; const bf16* wu = (const bf16*)d_in[7]; const bf16* wd = (const bf16*)d_in[8];
  const bf16* c = (const bf16*)d_in[9]; const bf16* wmod = (const bf16*)d_in[10];
  unsigned* mod = (unsigned*)((char*)d_ws + WS_MOD);
  k_pre<<<256, 512, 0, stream>>>(c, wmod, mod, (u32x4*)d_ws);
  k_main<<<256, 512, 0, stream>>>(x, K, V, wq, wo, wg, wu, wd, mod, (ull*)d_ws, (bf16*)d_out);
}
```
